```python
import torch

def _softmax(x, axis=-1):
    return torch.softmax(x, dim=axis)
def _gelu(x):
    return torch.nn.functional.gelu(x, approximate="tanh")
def _silu(x):
    return torch.nn.functional.silu(x)
def _sigmoid(x):
    return torch.sigmoid(x)
def _layernorm(x, axis=-1, eps=1e-5):
    m = x.mean(dim=axis, keepdim=True)
    v = x.var(dim=axis, keepdim=True, unbiased=False)
    return (x - m) / torch.sqrt(v + eps)
def _scatter_add(tgt, idx, src, axis=0):
    return tgt.clone().index_add_(axis, idx, src)
def _segment_sum(src, seg, n):
    out = torch.zeros((n,) + src.shape[1:], dtype=src.dtype)
    return out.index_add_(0, seg, src)
def _pad(x, pw):
    flat = tuple(v for pair in reversed(pw) for v in pair)
    return torch.nn.functional.pad(x, flat)

kernel_name = 'parallel_sum__linear_attn_block__dense_activation_squared_relu__prompt'

def setup_inputs():

    _w = (lambda a: (lambda t: t.to(torch.bfloat16) if t.is_floating_point() else t)(torch.as_tensor(a)))
    def _A_setup():
        B, S, D = 1, 2048, 576
        return {
            "x": _w((lambda *s: torch.randn(*s, dtype=torch.bfloat16) * 0.05)(B, S, D)),
            "wqkv": _w((lambda *s: torch.randn(*s, dtype=torch.bfloat16) * 0.4)(D, 3 * D)),
            "wo": _w((lambda *s: torch.randn(*s, dtype=torch.bfloat16) * 0.4)(D, D)),
        }
    _A_in = _A_setup()

    def _B_setup():
        B, S, D, F = 1, 2048, 576, 4608
        return {
            "x": _w((lambda *s: torch.randn(*s, dtype=torch.bfloat16) * 0.05)(B, S, D)),
            "w1": _w((lambda *s: torch.randn(*s, dtype=torch.bfloat16) * 0.05)(D, F)),
            "b1": _w((lambda *s: torch.randn(*s, dtype=torch.bfloat16) * 0.2)(F)),
            "w2": _w((lambda *s: torch.randn(*s, dtype=torch.bfloat16) * 0.05)(F, D)),
            "b2": _w((lambda *s: torch.randn(*s, dtype=torch.bfloat16) * 1.6)(D)),
        }
    _B_in = _B_setup()

    return {
        "A_x": _A_in["x"],
        "A_wqkv": _A_in["wqkv"],
        "A_wo": _A_in["wo"],
        "B_x": _B_in["x"],
        "B_w1": _B_in["w1"],
        "B_b1": _B_in["b1"],
        "B_w2": _B_in["w2"],
        "B_b2": _B_in["b2"],
    }

def reference(A_x, A_wqkv, A_wo, B_x, B_w1, B_b1, B_w2, B_b2):
    def _A_ref(x, wqkv, wo):
        B, S, D = x.shape
        H = 32; Dh = D // H
        qkv = (lambda x, *d: x.permute(*d))((x @ wqkv).reshape(B, S, 3, H, Dh), 2, 0, 3, 1, 4)
        q, k, v = qkv[0], qkv[1], qkv[2]
        phi = lambda t: torch.where(t > 0, t + 1, torch.exp(t)) + 1e-6
        pq, pk = phi(q), phi(k)
        kv = torch.einsum("bhsd,bhse->bhde", pk, v)
        z = torch.einsum("bhsd->bhd", pk)
        num = torch.einsum("bhqd,bhde->bhqe", pq, kv)
        den = torch.einsum("bhqd,bhd->bhq", pq, z)[..., None] + 1e-6
        o = num / den
        return (lambda x, *d: x.permute(*d))(o, 0, 2, 1, 3).reshape(B, S, D) @ wo

    def _B_ref(x, w1, b1, w2, b2):
        h = torch.einsum("bsd,df->bsf", x, w1) + b1
        zero = torch.zeros_like(h)
        r = torch.where(h > zero, h, zero)
        h = r * r
        out = torch.einsum("bsf,fd->bsd", h, w2) + b2
        return out

    x = A_x
    xn = _layernorm(x, axis=-1)
    return x + _A_ref(xn, A_wqkv, A_wo) + _B_ref(xn, B_w1, B_b1, B_w2, B_b2)
```

```cpp
#include <hip/hip_runtime.h>
#include <hip/hip_bf16.h>
#include <stdint.h>

typedef float f32x4 __attribute__((ext_vector_type(4)));
typedef float f32x2 __attribute__((ext_vector_type(2)));
typedef unsigned u32x4 __attribute__((ext_vector_type(4)));
typedef unsigned u32x2 __attribute__((ext_vector_type(2)));
typedef __bf16 bf16x8 __attribute__((ext_vector_type(8)));
typedef __bf16 bf16x2 __attribute__((ext_vector_type(2)));
typedef unsigned long long u64;
#define DI __device__ __forceinline__
#define SB() __builtin_amdgcn_sched_barrier(0)
#define GAS __attribute__((address_space(1)))
#define LAS __attribute__((address_space(3)))
#define RLX_AGENT __ATOMIC_RELAXED, __HIP_MEMORY_SCOPE_AGENT

constexpr int SEQ = 2048, DM = 576, FF = 4608;
constexpr unsigned ACT_OFF = 0;
constexpr unsigned PQ_KS = 18;
constexpr unsigned RING_OFF = 86016;
constexpr unsigned SLOT = 5120, RING_W = 3 * SLOT;
constexpr unsigned KVS_OFF = RING_OFF + 4 * RING_W;
constexpr unsigned LDS_BYTES = KVS_OFF + 16384;
static_assert(LDS_BYTES == 163840, "lds");
constexpr unsigned WS_EPOCH = 0;
constexpr unsigned WS_FKV = 4096;
constexpr unsigned WS_FFIN = WS_FKV + 8 * 32 * 2 * 128;
constexpr unsigned WS_FOUT = WS_FFIN + 8 * 16 * 128;
constexpr unsigned WS_KVP = WS_FOUT + 32 * 8 * 2 * 4 * 128;
constexpr unsigned WS_KVF = WS_KVP + 8 * 32 * 16 * 1024;
constexpr unsigned WS_OS = WS_KVF + 8 * 16 * 1024;
constexpr unsigned WS_OX = WS_OS + 32 * 8 * 2 * 4 * 8 * 1024;
constexpr unsigned WS_END = WS_OX + 32 * 8 * 4 * 2 * 1024;
constexpr unsigned OOB = 0x7F000000u;
constexpr unsigned SPIN_CAP = 1u << 16;

DI unsigned rfl(unsigned x) { return __builtin_amdgcn_readfirstlane(x); }
DI unsigned cvtpk(float lo, float hi) { f32x2 v = {lo, hi}; bf16x2 b = __builtin_convertvector(v, bf16x2); return __builtin_bit_cast(unsigned, b); }
DI float bflo(unsigned u) { return __uint_as_float(u << 16); }
DI float bfhi(unsigned u) { return __uint_as_float(u & 0xffff0000u); }
DI float phi(float t) { return (t > 0.f ? t + 1.f : __expf(t)) + 1e-6f; }

template <int OFF> DI void tr_rd(u32x2& d, unsigned a) { asm volatile("ds_read_b64_tr_b16 %0, %1 offset:%2" : "=v"(d) : "v"(a), "i"(OFF) : "memory"); }
template <int OFF> DI void b_rd(u32x4& d, unsigned a) { asm volatile("ds_read_b128 %0, %1 offset:%2" : "=v"(d) : "v"(a), "i"(OFF) : "memory"); }
template <bool FIRST> DI void mfma_a(f32x4& c, const u32x4& a, const u32x4& b) {
  if constexpr (FIRST) asm volatile("v_mfma_f32_16x16x32_bf16 %0, %1, %2, 0" : "=&v"(c) : "v"(a), "v"(b));
  else asm volatile("v_mfma_f32_16x16x32_bf16 %0, %1, %2, %0" : "+v"(c) : "v"(a), "v"(b));
}
DI f32x4 mfma_b(const u32x4& a, const u32x4& b, const f32x4& c) {
  return __builtin_amdgcn_mfma_f32_16x16x32_bf16(__builtin_bit_cast(bf16x8, a), __builtin_bit_cast(bf16x8, b), c, 0, 0, 0);
}
DI void barrier_lds() { asm volatile("s_waitcnt lgkmcnt(0)\n\ts_barrier" ::: "memory"); }

struct Frag { u32x2 lo, hi; };
DI u32x4 cat(const Frag& f) { return u32x4{f.lo[0], f.lo[1], f.hi[0], f.hi[1]}; }

struct Stream {
  u32x4 srd;
  unsigned soff, sstep;
  int left;
  int n5;
  unsigned vo[5];
  unsigned wslot;
  unsigned ring;
};
DI void dma_one(const Stream& s, int m) {
  const unsigned m0 = s.ring + s.wslot + m * 1024;
  asm volatile("s_mov_b32 m0, %0\n\ts_nop 0\n\tbuffer_load_dwordx4 %1, %2, %3 offen lds" :: "s"(m0), "v"(s.vo[m]), "s"(s.srd), "s"(s.soff) : "memory");
}
DI u32x4 make_srd(const void* p, unsigned bytes) {
  const u64 a = (u64)p;
  return u32x4{rfl((unsigned)a), rfl((unsigned)(a >> 32) & 0xffffu), rfl(bytes), 0x00020000u};
}
DI void seg4(Stream& s, const void* p, unsigned bytes, unsigned soff0, unsigned stride, int n, int lane) {
  s.srd = make_srd(p, bytes); s.soff = rfl(soff0); s.sstep = 32 * stride; s.left = n; s.n5 = 0;
  const unsigned base = (lane >> 3) * stride + (((lane & 7) ^ (((lane >> 4) & 3) << 1)) << 4);
#pragma unroll
  for (int m = 0; m < 4; ++m) s.vo[m] = base + 8 * m * stride;
  s.vo[4] = OOB;
}
DI void seg5(Stream& s, const void* p, unsigned bytes, unsigned soff0, unsigned stride, int n, int n1, int n2, unsigned alt, int lane) {
  s.srd = make_srd(p, bytes); s.soff = rfl(soff0); s.sstep = 32 * stride; s.left = n; s.n5 = 1;
#pragma unroll
  for (int m = 0; m < 5; ++m) {
    const int c = 64 * m + lane, row = (c * 6554) >> 16, cc = c - 10 * row;
    const unsigned col = cc < n1 ? cc * 16 : alt + (cc - n1) * 16;
    s.vo[m] = cc < n2 ? row * stride + col : OOB;
  }
}
DI void stream_fetch_all(Stream& s) {
#pragma unroll
  for (int m = 0; m < 4; ++m) dma_one(s, m);
  if (s.n5) dma_one(s, 4);
}
DI void stream_step(Stream& s) { s.soff += s.sstep; s.left -= 1; s.wslot = (s.wslot == 2 * SLOT) ? 0u : s.wslot + SLOT; }

struct Regs { Frag A[2][5]; u32x4 B[2][4]; };

template <int T, bool SWAP, bool FIRST, int X>
DI void unit(f32x4 (&acc)[5][4], Regs& R, Stream& s, const unsigned (&ta)[4], unsigned vb_next) {
  constexpr int Y = X ^ 1;
  const unsigned rs = s.ring + ((s.wslot == 2 * SLOT) ? 0u : s.wslot + SLOT);
  unsigned ra[4];
  if constexpr (T == 4) { ra[0] = ta[0] + rs; ra[1] = ta[1] + rs; ra[2] = ta[2] + rs; ra[3] = ta[3] + rs; }
  else { ra[0] = ta[0] + rs; }
  SB();
#pragma unroll
  for (int nt = 0; nt < T; ++nt) {
    const u32x4 a = cat(R.A[X][nt]);
#pragma unroll
    for (int st = 0; st < 4; ++st) {
      constexpr int G0 = T;
      const int gi = nt * 4 + st;
      if (gi == G0) asm volatile("s_waitcnt vmcnt(8)" ::: "memory");
      if constexpr (SWAP) mfma_a<FIRST>(acc[nt][st], R.B[X][st], a); else mfma_a<FIRST>(acc[nt][st], a, R.B[X][st]);
      if (gi < 4) {
        dma_one(s, gi);
        if (gi == 0) b_rd<0>(R.B[Y][0], vb_next);
        if (gi == 1) b_rd<1024>(R.B[Y][1], vb_next);
        if (gi == 2) b_rd<2048>(R.B[Y][2], vb_next);
        if (gi == 3) b_rd<3072>(R.B[Y][3], vb_next);
      }
      if (gi == 4) { if (s.n5) dma_one(s, 4); }
      if (gi >= G0 && gi < G0 + 2 * T) {
        const int r = gi - G0, n2 = r >> 1;
        if constexpr (T == 4) {
          if (r == 0) tr_rd<0>(R.A[Y][0].lo, ra[0]); if (r == 1) tr_rd<2048>(R.A[Y][0].hi, ra[0]);
          if (r == 2) tr_rd<0>(R.A[Y][1].lo, ra[1]); if (r == 3) tr_rd<2048>(R.A[Y][1].hi, ra[1]);
          if (r == 4) tr_rd<0>(R.A[Y][2].lo, ra[2]); if (r == 5) tr_rd<2048>(R.A[Y][2].hi, ra[2]);
          if (r == 6) tr_rd<0>(R.A[Y][3].lo, ra[3]); if (r == 7) tr_rd<2048>(R.A[Y][3].hi, ra[3]);
        } else {
          if (r == 0) tr_rd<0>(R.A[Y][0].lo, ra[0]);   if (r == 1) tr_rd<2560>(R.A[Y][0].hi, ra[0]);
          if (r == 2) tr_rd<32>(R.A[Y][1].lo, ra[0]);  if (r == 3) tr_rd<2592>(R.A[Y][1].hi, ra[0]);
          if (r == 4) tr_rd<64>(R.A[Y][2].lo, ra[0]);  if (r == 5) tr_rd<2624>(R.A[Y][2].hi, ra[0]);
          if (r == 6) tr_rd<96>(R.A[Y][3].lo, ra[0]);  if (r == 7) tr_rd<2656>(R.A[Y][3].hi, ra[0]);
          if (r == 8) tr_rd<128>(R.A[Y][4].lo, ra[0]); if (r == 9) tr_rd<2688>(R.A[Y][4].hi, ra[0]);
        }
        (void)n2;
      }
    }
    SB();
  }
  asm volatile("s_waitcnt lgkmcnt(0)" ::: "memory");
  SB();
  stream_step(s);
}

template <int T, bool SWAP, class Next, class Hook>
DI void pass(f32x4 (&acc)[5][4], Stream& s, const unsigned (&ta)[4], int n, int lane, Next next, Hook hook) {
  Regs R;
  const unsigned vb0 = ACT_OFF + lane * 16;
  {
    asm volatile("s_waitcnt vmcnt(8)" ::: "memory");
    const unsigned rs = s.ring + s.wslot;
    if constexpr (T == 4) {
#pragma unroll
      for (int nt = 0; nt < 4; ++nt) { tr_rd<0>(R.A[0][nt].lo, ta[nt] + rs); tr_rd<2048>(R.A[0][nt].hi, ta[nt] + rs); }
    } else {
      const unsigned a0 = ta[0] + rs;
      tr_rd<0>(R.A[0][0].lo, a0);   tr_rd<2560>(R.A[0][0].hi, a0);
      tr_rd<32>(R.A[0][1].lo, a0);  tr_rd<2592>(R.A[0][1].hi, a0);
      tr_rd<64>(R.A[0][2].lo, a0);  tr_rd<2624>(R.A[0][2].hi, a0);
      tr_rd<96>(R.A[0][3].lo, a0);  tr_rd<2656>(R.A[0][3].hi, a0);
      tr_rd<128>(R.A[0][4].lo, a0); tr_rd<2688>(R.A[0][4].hi, a0);
    }
    b_rd<0>(R.B[0][0], vb0); b_rd<1024>(R.B[0][1], vb0); b_rd<2048>(R.B[0][2], vb0); b_rd<3072>(R.B[0][3], vb0);
    asm volatile("s_waitcnt lgkmcnt(0)" ::: "memory");
    SB();
  }
  unit<T, SWAP, true, 0>(acc, R, s, ta, vb0 + 4096);
  if (s.left == 0) next();
  int u = 1;
  while (true) {
    unit<T, SWAP, false, 1>(acc, R, s, ta, vb0 + (u + 1 < n ? (u + 1) * 4096 : 0));
    if (s.left == 0) next();
    hook(u);
    if (++u == n) break;
    unit<T, SWAP, false, 0>(acc, R, s, ta, vb0 + (u + 1 < n ? (u + 1) * 4096 : 0));
    if (s.left == 0) next();
    hook(u);
    if (++u == n) break;
  }
  asm volatile("s_nop 7\n\ts_nop 7" ::: "memory");
  SB();
}

DI bool poll_flags(const GAS u64* p, bool active, u64 tag) {
  for (unsigned spins = 0; spins < SPIN_CAP; ++spins) {
    u64 v = tag;
    if (active) v = __hip_atomic_load(p, RLX_AGENT);
    if (__all(v == tag)) return true;
    __builtin_amdgcn_s_sleep(1);
  }
  return false;
}
DI void set_flag(GAS u64* p, u64 tag, int lane) { if (lane == 0) __hip_atomic_store(p, tag, RLX_AGENT); }

DI void h_epilogue(const f32x4 (&acc)[5][4], const u32x2 (&bias)[4], u32x4 (&h)[2][4]) {
#pragma unroll
  for (int m = 0; m < 2; ++m)
#pragma unroll
    for (int st = 0; st < 4; ++st) {
      u32x4 r;
#pragma unroll
      for (int e = 0; e < 2; ++e) {
        const int nt = 2 * m + e;
        float v0 = fmaxf(acc[nt][st][0] + bflo(bias[nt][0]), 0.f), v1 = fmaxf(acc[nt][st][1] + bfhi(bias[nt][0]), 0.f);
        float v2 = fmaxf(acc[nt][st][2] + bflo(bias[nt][1]), 0.f), v3 = fmaxf(acc[nt][st][3] + bfhi(bias[nt][1]), 0.f);
        r[2 * e] = cvtpk(v0 * v0, v1 * v1); r[2 * e + 1] = cvtpk(v2 * v2, v3 * v3);
      }
      h[m][st] = r;
    }
}

__global__ __launch_bounds__(256, 1) void fused_block_kernel(const uint16_t* __restrict__ x, const uint16_t* __restrict__ wqkv, const uint16_t* __restrict__ wo,
                                                              const uint16_t* __restrict__ w1, const uint16_t* __restrict__ b1, const uint16_t* __restrict__ w2,
                                                              const uint16_t* __restrict__ b2, uint16_t* __restrict__ out, char* __restrict__ ws) {
  extern __shared__ __attribute__((aligned(16))) char smem[];
  LAS char* lds = (LAS char*)smem;
  const int tid = threadIdx.x, lane = tid & 63;
  const int w = rfl(tid >> 6);
  const int j = blockIdx.x & 7, i = blockIdx.x >> 3;
  const int g = lane >> 4, l15 = lane & 15;
  GAS char* wsg = (GAS char*)ws;
  const __amdgpu_buffer_rsrc_t wsr = __builtin_amdgcn_make_buffer_rsrc((void*)ws, 0, WS_END, 0x00020000);

  const int xr = lane >> 2, xc = lane & 3;
  u32x4 xv[18];
  {
    const GAS u32x4* xp = (const GAS u32x4*)(x + (size_t)(64 * i + 16 * w + xr) * DM + 8 * xc);
#pragma unroll
    for (int k = 0; k < 18; ++k) xv[k] = xp[4 * k];
  }
  Stream s;
  s.ring = RING_OFF + w * RING_W; s.wslot = 0;
  if (w < 2) seg5(s, wqkv, DM * 3456, 1152 + 144 * j + 72 * w, 3456, 18, 5, 10, 1152, lane);
  else if (w == 2) seg5(s, wqkv, DM * 3456, 144 * j, 3456, 18, 9, 9, 0, lane);
  else seg5(s, w1, DM * 9216, (576 * j + 512) * 2, 9216, 18, 8, 8, 0, lane);
  for (int u = 0; u < 3; ++u) { stream_fetch_all(s); stream_step(s); }
  const u64 tag = __hip_atomic_load((const GAS u64*)(wsg + WS_EPOCH), RLX_AGENT) + 1;
  u32x2 bias[3][4];
#pragma unroll
  for (int nt = 0; nt < 4; ++nt) {
    bias[0][nt] = *(const GAS u32x2*)(b1 + 576 * j + 128 * w + 16 * nt + 4 * g);
    bias[1][nt] = *(const GAS u32x2*)(b1 + 576 * j + 128 * w + 64 + 16 * nt + 4 * g);
    bias[2][nt] = *(const GAS u32x2*)(b1 + 576 * j + 512 + 16 * nt + 4 * g);
  }
  {
    unsigned sink = 0;
    if (tid < 216) { const int r = tid / 12, q = tid % 12; sink += *(const GAS unsigned*)((const GAS char*)wqkv + (18 * i + r) * 3456 + (q >> 2) * 1152 + ((144 * j) & ~63) + (q & 3) * 64 - (((144 * j) & ~63) + (q & 3) * 64 >= 1152 ? 64 : 0)); }
    for (int e = tid; e < 324; e += 256) {
      const int r = e / 18, q = e % 18;
      sink += *(const GAS unsigned*)((const GAS char*)w1 + (18 * i + r) * 9216 + 1152 * j + q * 64);
      sink += *(const GAS unsigned*)((const GAS char*)w2 + (size_t)(576 * j + 18 * i + r) * 1152 + q * 64);
    }
    if (tid < 41) { const int e = i * 41 + tid; if (e < 72 * 18) sink += *(const GAS unsigned*)((const GAS char*)wo + 72 * j * 1152 + e * 64); }
    asm volatile("" :: "v"(sink));
  }
  {
    float sum = 0.f;
#pragma unroll
    for (int k = 0; k < 18; ++k)
#pragma unroll
      for (int e = 0; e < 4; ++e) sum += bflo(xv[k][e]) + bfhi(xv[k][e]);
    sum += __shfl_xor(sum, 1); sum += __shfl_xor(sum, 2);
    const float mean = sum * (1.f / DM);
    float sq = 0.f;
#pragma unroll
    for (int k = 0; k < 18; ++k)
#pragma unroll
      for (int e = 0; e < 4; ++e) { const float a = bflo(xv[k][e]) - mean, b = bfhi(xv[k][e]) - mean; sq += a * a + b * b; }
    sq += __shfl_xor(sq, 1); sq += __shfl_xor(sq, 2);
    const float rstd = 1.f / sqrtf(sq * (1.f / DM) + 1e-5f);
    const unsigned wa = ACT_OFF + w * 1024 + (xr + 16 * ((2 * xc) & 3)) * 16 + (xc >> 1) * 8;
#pragma unroll
    for (int k = 0; k < 18; ++k) {
      u32x2 p0, p1;
      p0[0] = cvtpk((bflo(xv[k][0]) - mean) * rstd, (bfhi(xv[k][0]) - mean) * rstd);
      p0[1] = cvtpk((bflo(xv[k][1]) - mean) * rstd, (bfhi(xv[k][1]) - mean) * rstd);
      p1[0] = cvtpk((bflo(xv[k][2]) - mean) * rstd, (bfhi(xv[k][2]) - mean) * rstd);
      p1[1] = cvtpk((bflo(xv[k][3]) - mean) * rstd, (bfhi(xv[k][3]) - mean) * rstd);
      *(LAS u32x2*)(lds + wa + k * 4096) = p0;
      *(LAS u32x2*)(lds + wa + k * 4096 + 256) = p1;
    }
  }
  unsigned ta4[4], ta5[4];
  {
    const int q = l15 >> 2, p = lane & 3, row = 4 * g + q;
#pragma unroll
    for (int nt = 0; nt < 4; ++nt) ta4[nt] = row * 128 + ((nt ^ ((row >> 1) & 3)) * 32) + 8 * p;
    ta5[0] = row * 160 + 8 * p; ta5[1] = ta5[2] = ta5[3] = 0;
  }
  barrier_lds();

  f32x4 acc[5][4];
  u32x4 hA[2][4], hB1[2][4], hB2[2][4];
  auto nohook = [](int) {};
  auto to_B1 = [&]() { seg4(s, w1, DM * 9216, (576 * j + 128 * w) * 2, 9216, 18, lane); };
  if (w < 2) {
    pass<5, true>(acc, s, ta5, 18, lane, to_B1, nohook);
    u32x4 PK[3][2], V[3][2];
#pragma unroll
    for (int p = 0; p < 2; ++p) {
#pragma unroll
      for (int nt = 0; nt < 3; ++nt) {
        PK[nt][p] = u32x4{cvtpk(phi(acc[nt][2 * p][0]), phi(acc[nt][2 * p][1])), cvtpk(phi(acc[nt][2 * p][2]), phi(acc[nt][2 * p][3])),
                          cvtpk(phi(acc[nt][2 * p + 1][0]), phi(acc[nt][2 * p + 1][1])), cvtpk(phi(acc[nt][2 * p + 1][2]), phi(acc[nt][2 * p + 1][3]))};
        const int n2 = nt + 2;
        u32x4 v = u32x4{cvtpk(acc[n2][2 * p][0], acc[n2][2 * p][1]), cvtpk(acc[n2][2 * p][2], acc[n2][2 * p][3]),
                        cvtpk(acc[n2][2 * p + 1][0], acc[n2][2 * p + 1][1]), cvtpk(acc[n2][2 * p + 1][2], acc[n2][2 * p + 1][3])};
        if (nt == 2 && l15 == 12) v = u32x4{0x3f803f80u, 0x3f803f80u, 0x3f803f80u, 0x3f803f80u};
        V[nt][p] = v;
      }
    }
    const unsigned pbase = WS_KVP + ((j * 32 + i) * 16 + w * 8) * 1024 + lane * 16;
    int t = 0;
#pragma unroll
    for (int dn = 0; dn < 3; ++dn)
#pragma unroll
      for (int en = 0; en < 3; ++en) {
        if (dn == 2 && en == 0) continue;
        f32x4 d = mfma_b(PK[dn][0], V[en][0], f32x4{0, 0, 0, 0});
        d = mfma_b(PK[dn][1], V[en][1], d);
        __builtin_amdgcn_raw_buffer_store_b128(__builtin_bit_cast(u32x4, d), wsr, pbase + t * 1024, 0, 16);
        ++t;
      }
  } else {
    pass<5, false>(acc, s, ta5, 18, lane, to_B1, nohook);
    if (w == 2) {
#pragma unroll
      for (int st = 0; st < 4; ++st) {
#pragma unroll
        for (int m = 0; m < 3; ++m) {
          u32x4 r;
          r[0] = cvtpk(phi(acc[2 * m][st][0]), phi(acc[2 * m][st][1])); r[1] = cvtpk(phi(acc[2 * m][st][2]), phi(acc[2 * m][st][3]));
          if (m < 2) { r[2] = cvtpk(phi(acc[2 * m + 1][st][0]), phi(acc[2 * m + 1][st][1])); r[3] = cvtpk(phi(acc[2 * m + 1][st][2]), phi(acc[2 * m + 1][st][3])); }
          else { r[2] = 0; r[3] = 0; }
          *(LAS u32x4*)(lds + ACT_OFF + ((PQ_KS + m) * 4 + st) * 1024 + lane * 16) = r;
        }
      }
    } else {
      h_epilogue(acc, bias[2], hA);
    }
  }
  {
    auto to_B2 = [&]() { seg4(s, w1, DM * 9216, (576 * j + 128 * w + 64) * 2, 9216, 18, lane); };
    auto hook = [&](int u) { if (u == 3 && w < 2) set_flag((GAS u64*)(wsg + WS_FKV + ((j * 32 + i) * 2 + w) * 128), tag, lane); };
    pass<4, false>(acc, s, ta4, 18, lane, to_B2, hook);
    h_epilogue(acc, bias[0], hB1);
  }
  if (w == 3 && i < 16) {
    poll_flags((const GAS u64*)(wsg + WS_FKV + ((j * 32 + (lane & 31)) * 2 + (i >> 3)) * 128), true, tag);
    f32x4 sum = {0, 0, 0, 0};
    u32x4 tv[32];
#pragma unroll
    for (int q = 0; q < 32; ++q) tv[q] = __builtin_amdgcn_raw_buffer_load_b128(wsr, WS_KVP + ((j * 32 + q) * 16 + i) * 1024 + lane * 16, 0, 16);
#pragma unroll
    for (int q = 0; q < 32; ++q) sum += __builtin_bit_cast(f32x4, tv[q]);
    __builtin_amdgcn_raw_buffer_store_b128(__builtin_bit_cast(u32x4, sum), wsr, WS_KVF + (j * 16 + i) * 1024 + lane * 16, 0, 16);
    asm volatile("s_waitcnt vmcnt(0)" ::: "memory");
    set_flag((GAS u64*)(wsg + WS_FFIN + (j * 16 + i) * 128), tag, lane);
  }
  {
    auto to_C1 = [&]() { seg5(s, w2, FF * 1152, 576 * j * 1152 + w * 128, 1152, 18, 8, 10, (8 - w) * 128 + 32 * w, lane); };
    pass<4, false>(acc, s, ta4, 18, lane, to_C1, nohook);
    h_epilogue(acc, bias[1], hB2);
  }
  {
    poll_flags((const GAS u64*)(wsg + WS_FFIN + (j * 16 + 4 * w + (lane & 3)) * 128), true, tag);
    u32x4 kt[4];
#pragma unroll
    for (int q = 0; q < 4; ++q) kt[q] = __builtin_amdgcn_raw_buffer_load_b128(wsr, WS_KVF + (j * 16 + 4 * w + q) * 1024 + lane * 16, 0, 16);
    barrier_lds();
#pragma unroll
    for (int m = 0; m < 2; ++m)
#pragma unroll
      for (int st = 0; st < 4; ++st) {
        *(LAS u32x4*)(lds + ACT_OFF + ((4 * w + m) * 4 + st) * 1024 + lane * 16) = hB1[m][st];
        *(LAS u32x4*)(lds + ACT_OFF + ((4 * w + 2 + m) * 4 + st) * 1024 + lane * 16) = hB2[m][st];
        if (w == 3) *(LAS u32x4*)(lds + ACT_OFF + ((16 + m) * 4 + st) * 1024 + lane * 16) = hA[m][st];
      }
#pragma unroll
    for (int q = 0; q < 4; ++q) *(LAS u32x4*)(lds + KVS_OFF + (4 * w + q) * 1024 + lane * 16) = kt[q];
    barrier_lds();
    u32x2 ores[2][4];
#pragma unroll
    for (int pass2 = 0; pass2 < 2; ++pass2) {
      if (pass2 == 1 && w != 0) break;
      const int et = pass2 == 1 ? 4 : w;
      const int e = 16 * et + l15, He = (e * 3641) >> 16;
      const int hp = (He >> 1) & 1, c = 40 + e - 36 * hp;
      const int ks0 = et < 3 ? 0 : 1;
      f32x4 num[4], den[4];
#pragma unroll
      for (int st = 0; st < 4; ++st) { num[st] = f32x4{0, 0, 0, 0}; den[st] = f32x4{0, 0, 0, 0}; }
#pragma unroll
      for (int kk = 0; kk < 2; ++kk) {
        const int ks = ks0 + kk;
        u32x4 fk, fz;
#pragma unroll
        for (int half = 0; half < 2; ++half) {
          const int db = 32 * ks + 16 * half + 4 * g;
          int dp = db - 36 * hp; dp = dp < 0 ? 0 : (dp > 32 ? 32 : dp);
          const int dn = dp >> 4, en = (c >> 4) > 4 ? 4 : (c >> 4);
          const int idx = dn * 3 + (en - 2) - (dn == 2 ? 1 : 0), idz = dn * 3 + 2 - (dn == 2 ? 1 : 0);
          const f32x4 kv4 = *(const LAS f32x4*)(lds + KVS_OFF + (hp * 8 + idx) * 1024 + ((c & 15) + 16 * ((dp & 15) >> 2)) * 16);
          const f32x4 z4 = *(const LAS f32x4*)(lds + KVS_OFF + (hp * 8 + idz) * 1024 + (12 + 16 * ((dp & 15) >> 2)) * 16);
          float a[4], b[4];
#pragma unroll
          for (int r = 0; r < 4; ++r) { const int d = db + r; const bool ok = e < 72 && d >= 18 * He && d < 18 * He + 18; a[r] = ok ? kv4[r] : 0.f; b[r] = ok ? z4[r] : 0.f; }
          fk[2 * half] = cvtpk(a[0], a[1]); fk[2 * half + 1] = cvtpk(a[2], a[3]);
          fz[2 * half] = cvtpk(b[0], b[1]); fz[2 * half + 1] = cvtpk(b[2], b[3]);
        }
#pragma unroll
        for (int st = 0; st < 4; ++st) {
          const u32x4 pq = *(const LAS u32x4*)(lds + ACT_OFF + ((PQ_KS + ks) * 4 + st) * 1024 + lane * 16);
          num[st] = mfma_b(fk, pq, num[st]); den[st] = mfma_b(fz, pq, den[st]);
        }
      }
#pragma unroll
      for (int st = 0; st < 4; ++st) {
        float o[4];
#pragma unroll
        for (int r = 0; r < 4; ++r) o[r] = num[st][r] / (den[st][r] + 1e-6f);
        ores[pass2][st] = u32x2{cvtpk(o[0], o[1]), cvtpk(o[2], o[3])};
      }
    }
    barrier_lds();
#pragma unroll
    for (int st = 0; st < 4; ++st) {
      *(LAS u32x2*)(lds + ACT_OFF + ((PQ_KS + (w >> 1)) * 4 + st) * 1024 + lane * 16 + (w & 1) * 8) = ores[0][st];
      if (w == 0) *(LAS u32x2*)(lds + ACT_OFF + ((PQ_KS + 2) * 4 + st) * 1024 + lane * 16) = ores[1][st];
    }
    barrier_lds();
  }
  int cseg = 0;
  {
    auto nxt = [&]() {
      if (cseg == 0) seg5(s, wo, (72 * j + 72) * 1152, 72 * j * 1152 + w * 128, 1152, 3, 8, 10, (8 - w) * 128 + 32 * w, lane);
      else seg4(s, w2, FF * 1152, 576 * j * 1152 + (4 + w) * 128, 1152, 18, lane);
      ++cseg;
    };
    pass<5, false>(acc, s, ta5, 21, lane, nxt, nohook);
    const unsigned sb = WS_OS + (((i * 8 + j) * 2 + 0) * 4 + w) * 8 * 1024 + lane * 16;
#pragma unroll
    for (int d = 0; d < 8; ++d) {
      const int n0 = 2 * (d >> 2), st = d & 3;
      const u32x4 v = {cvtpk(acc[n0][st][0], acc[n0][st][1]), cvtpk(acc[n0][st][2], acc[n0][st][3]), cvtpk(acc[n0 + 1][st][0], acc[n0 + 1][st][1]), cvtpk(acc[n0 + 1][st][2], acc[n0 + 1][st][3])};
      __builtin_amdgcn_raw_buffer_store_b128(v, wsr, sb + d * 1024, 0, 16);
    }
#pragma unroll
    for (int p = 0; p < 2; ++p) {
      const u32x4 v = {cvtpk(acc[4][2 * p][0], acc[4][2 * p][1]), cvtpk(acc[4][2 * p][2], acc[4][2 * p][3]), cvtpk(acc[4][2 * p + 1][0], acc[4][2 * p + 1][1]), cvtpk(acc[4][2 * p + 1][2], acc[4][2 * p + 1][3])};
      __builtin_amdgcn_raw_buffer_store_b128(v, wsr, WS_OX + (((i * 8 + j) * 4 + w) * 2 + p) * 1024 + lane * 16, 0, 16);
    }
  }
  {
    cseg = 0;
    auto nxt = [&]() {
      if (cseg == 0) seg4(s, wo, (72 * j + 72) * 1152, 72 * j * 1152 + (4 + w) * 128, 1152, 3, lane);
      else seg4(s, wo, 0, 0, 0, 1000, lane);
      ++cseg;
    };
    auto hook = [&](int u) { if (u == 3) set_flag((GAS u64*)(wsg + WS_FOUT + (((i * 8 + j) * 2 + 0) * 4 + w) * 128), tag, lane); };
    pass<4, false>(acc, s, ta4, 21, lane, nxt, hook);
    const unsigned sb = WS_OS + (((i * 8 + j) * 2 + 1) * 4 + w) * 8 * 1024 + lane * 16;
#pragma unroll
    for (int d = 0; d < 8; ++d) {
      const int n0 = 2 * (d >> 2), st = d & 3;
      const u32x4 v = {cvtpk(acc[n0][st][0], acc[n0][st][1]), cvtpk(acc[n0][st][2], acc[n0][st][3]), cvtpk(acc[n0 + 1][st][0], acc[n0 + 1][st][1]), cvtpk(acc[n0 + 1][st][2], acc[n0 + 1][st][3])};
      __builtin_amdgcn_raw_buffer_store_b128(v, wsr, sb + d * 1024, 0, 16);
    }
    asm volatile("s_waitcnt vmcnt(0)" ::: "memory");
    set_flag((GAS u64*)(wsg + WS_FOUT + (((i * 8 + j) * 2 + 1) * 4 + w) * 128), tag, lane);
  }
  {
    auto finish = [&](const u32x4 (&t)[8], int dt0, int st0, int dt1, int st1) {
      float v[8] = {0, 0, 0, 0, 0, 0, 0, 0};
#pragma unroll
      for (int q = 0; q < 8; ++q) {
#pragma unroll
        for (int e = 0; e < 4; ++e) { v[2 * e] += bflo(t[q][e]); v[2 * e + 1] += bfhi(t[q][e]); }
      }
#pragma unroll
      for (int h = 0; h < 2; ++h) {
        const int dt = h ? dt1 : dt0, st = h ? st1 : st0;
        const size_t o = (size_t)(64 * i + 16 * st + l15) * DM + 16 * dt + 4 * g;
        const u32x2 xr2 = *(const GAS u32x2*)(x + o), bb = *(const GAS u32x2*)(b2 + 16 * dt + 4 * g);
        const u32x2 r = {cvtpk(v[4 * h] + bflo(xr2[0]) + bflo(bb[0]), v[4 * h + 1] + bfhi(xr2[0]) + bfhi(bb[0])),
                         cvtpk(v[4 * h + 2] + bflo(xr2[1]) + bflo(bb[1]), v[4 * h + 3] + bfhi(xr2[1]) + bfhi(bb[1]))};
        *(GAS u32x2*)(out + o) = r;
      }
    };
    u32x4 t[8];
    poll_flags((const GAS u64*)(wsg + WS_FOUT + (((i * 8 + (lane & 7)) * 2 + 0) * 4 + w) * 128), true, tag);
#pragma unroll
    for (int q = 0; q < 8; ++q) t[q] = __builtin_amdgcn_raw_buffer_load_b128(wsr, WS_OS + ((((i * 8 + q) * 2 + 0) * 4 + w) * 8 + j) * 1024 + lane * 16, 0, 16);
    finish(t, 4 * w + 2 * (j >> 2), j & 3, 4 * w + 2 * (j >> 2) + 1, j & 3);
    if (w == (j >> 1)) {
#pragma unroll
      for (int q = 0; q < 8; ++q) t[q] = __builtin_amdgcn_raw_buffer_load_b128(wsr, WS_OX + (((i * 8 + q) * 4 + w) * 2 + (j & 1)) * 1024 + lane * 16, 0, 16);
      finish(t, 32 + w, 2 * (j & 1), 32 + w, 2 * (j & 1) + 1);
    }
    poll_flags((const GAS u64*)(wsg + WS_FOUT + (((i * 8 + (lane & 7)) * 2 + 1) * 4 + w) * 128), true, tag);
#pragma unroll
    for (int q = 0; q < 8; ++q) t[q] = __builtin_amdgcn_raw_buffer_load_b128(wsr, WS_OS + ((((i * 8 + q) * 2 + 1) * 4 + w) * 8 + j) * 1024 + lane * 16, 0, 16);
    finish(t, 16 + 4 * w + 2 * (j >> 2), j & 3, 16 + 4 * w + 2 * (j >> 2) + 1, j & 3);
  }
  if (blockIdx.x == 0 && tid == 0) __hip_atomic_store((GAS u64*)(wsg + WS_EPOCH), tag, RLX_AGENT);
}

extern "C" void kernel_launch(void* const* d_in, const int* in_sizes, int n_in, void* d_out, int out_size, void* d_ws, size_t ws_size, hipStream_t stream) {
  (void)in_sizes; (void)n_in; (void)out_size; (void)ws_size;
  (void)hipFuncSetAttribute((const void*)fused_block_kernel, hipFuncAttributeMaxDynamicSharedMemorySize, LDS_BYTES);
  fused_block_kernel<<<256, 256, LDS_BYTES, stream>>>((const uint16_t*)d_in[0], (const uint16_t*)d_in[1], (const uint16_t*)d_in[2], (const uint16_t*)d_in[4],
                                                      (const uint16_t*)d_in[5], (const uint16_t*)d_in[6], (const uint16_t*)d_in[7], (uint16_t*)d_out, (char*)d_ws);
}
```

```cpp
#include <hip/hip_runtime.h>
#include <hip/hip_bf16.h>
#include <stdint.h>
#include <utility>

typedef float f32x4 __attribute__((ext_vector_type(4)));
typedef float f32x2 __attribute__((ext_vector_type(2)));
typedef unsigned u32x4 __attribute__((ext_vector_type(4)));
typedef unsigned u32x2 __attribute__((ext_vector_type(2)));
typedef __bf16 bf16x8 __attribute__((ext_vector_type(8)));
typedef __bf16 bf16x2 __attribute__((ext_vector_type(2)));
typedef unsigned long long u64;
#define DI __device__ __forceinline__
#define SB() __builtin_amdgcn_sched_barrier(0)
#define GAS __attribute__((address_space(1)))
#define LAS __attribute__((address_space(3)))
#define RLX_AGENT __ATOMIC_RELAXED, __HIP_MEMORY_SCOPE_AGENT
#define UNLIKELY(x) __builtin_expect(!!(x), 0)

constexpr int DM = 576, FF = 4608;
constexpr unsigned ACT_OFF = 0;
constexpr unsigned PQ_KS = 18;
constexpr unsigned RING_OFF = 86016;
constexpr unsigned SLOT = 5120, RING_W = 3 * SLOT;
constexpr unsigned SPARE_OFF = RING_OFF + 4 * RING_W;
constexpr unsigned B1_OFF = SPARE_OFF;
constexpr unsigned B2_OFF = SPARE_OFF + 2048;
constexpr unsigned XS_OFF = SPARE_OFF + 4096;
constexpr unsigned PQF_OFF = SPARE_OFF + 13312;
constexpr unsigned SCR_OFF = SPARE_OFF + 15360;
constexpr unsigned LDS_BYTES = SPARE_OFF + 16384;
static_assert(LDS_BYTES == 163840, "lds");
constexpr unsigned WS_EPOCH = 0;
constexpr unsigned WS_KVP = 4096;
constexpr unsigned WS_KVF = WS_KVP + 8 * 32 * 18 * 1024;
constexpr unsigned WS_OS = WS_KVF + 8 * 18 * 1024;
constexpr unsigned WS_OX = WS_OS + 32 * 8 * 2 * 4 * 8 * 1024;
constexpr unsigned WS_END = WS_OX + 32 * 8 * 4 * 2 * 1024;
#define CODE_LINES 384
constexpr int KV1_DELAY = 8, KV_EXTRA = 9;
constexpr unsigned OOB = 0x7F000000u;
constexpr unsigned SPIN_CAP = 1u << 13;

DI unsigned rfl(unsigned x) { return __builtin_amdgcn_readfirstlane(x); }
DI unsigned rfl_s(unsigned x) { unsigned r; asm volatile("s_nop 0\n\tv_readfirstlane_b32 %0, %1\n\ts_nop 4" : "=s"(r) : "v"(x)); return r; }
DI unsigned cvtpk(float lo, float hi) { f32x2 v = {lo, hi}; bf16x2 b = __builtin_convertvector(v, bf16x2); return __builtin_bit_cast(unsigned, b); }
DI float bflo(unsigned u) { return __uint_as_float(u << 16); }
DI float bfhi(unsigned u) { return __uint_as_float(u & 0xffff0000u); }
DI f32x4 phi4(const f32x4& t) {
  f32x4 r;
#pragma unroll
  for (int h = 0; h < 2; ++h) {
    const f32x2 x = {t[2 * h], t[2 * h + 1]}, u = x * f32x2{1.44269504088896f, 1.44269504088896f};
    const f32x2 e = {__builtin_amdgcn_exp2f(fminf(u[0], 0.f)), __builtin_amdgcn_exp2f(fminf(u[1], 0.f))};
    const f32x2 m = {fmaxf(x[0], 0.f), fmaxf(x[1], 0.f)}, y = (m + f32x2{1e-6f, 1e-6f}) + e;
    r[2 * h] = y[0]; r[2 * h + 1] = y[1];
  }
  return r;
}
template <int CTRL> DI float dpp_add(float v) { return v + __builtin_bit_cast(float, __builtin_amdgcn_update_dpp(0, __builtin_bit_cast(int, v), CTRL, 0xf, 0xf, true)); }
DI float sum8(float v) { v = dpp_add<0xB1>(v); v = dpp_add<0x4E>(v); return dpp_add<0x141>(v); }

typedef f32x4 Acc[5][4];
template <int NT, int ST, bool FIRST> DI void mfma_acc(Acc& acc, const u32x4& a, const u32x4& b) {
  f32x4& c = acc[NT][ST];
  asm volatile("v_mfma_f32_16x16x32_bf16 %0, %1, %2, %0" : "+v"(c) : "v"(a), "v"(b));
}

template <int T> DI void acc_zero(Acc& acc) {
#pragma unroll
  for (int nt = 0; nt < T; ++nt)
#pragma unroll
    for (int st = 0; st < 4; ++st) { acc[nt][st] = f32x4{0.f, 0.f, 0.f, 0.f}; asm volatile("" : "+v"(acc[nt][st])); }
}
DI void acc_bias(Acc& acc, const LAS char* bp) {
#pragma unroll
  for (int nt = 0; nt < 4; ++nt) {
    const u32x2 b = *(const LAS u32x2*)(bp + nt * 32);
    const f32x4 f = {__uint_as_float(b[0] << 16), __uint_as_float(b[0] & 0xffff0000u), __uint_as_float(b[1] << 16), __uint_as_float(b[1] & 0xffff0000u)};
#pragma unroll
    for (int st = 0; st < 4; ++st) { acc[nt][st] = f; asm volatile("" : "+v"(acc[nt][st])); }
  }
}
template <int OFF> DI void tr_rd(u32x2& d, unsigned a) {
  asm volatile("ds_read_b64_tr_b16 %0, %1 offset:%2" : "=v"(d) : "v"(a), "i"(OFF) : "memory");
}
template <int OFF> DI void b_rd(u32x4& d, unsigned a) {
  asm volatile("ds_read_b128 %0, %1 offset:%2" : "=v"(d) : "v"(a), "i"(OFF) : "memory");
}
DI f32x4 mfma_b(const u32x4& a, const u32x4& b, const f32x4& c) {
  return __builtin_amdgcn_mfma_f32_16x16x32_bf16(__builtin_bit_cast(bf16x8, a), __builtin_bit_cast(bf16x8, b), c, 0, 0, 0);
}
template <class V> DI void pin_v(V& v) { asm volatile("" : "+v"(v)); }
template <int OFF> DI void lds_wr128(unsigned addr, const u32x4& a) { asm volatile("ds_write_b128 %0, %1 offset:%2" :: "v"(addr), "v"(a), "i"(OFF) : "memory"); }
DI void sync4(LAS char* lds, unsigned off, unsigned n, int lane) {
  asm volatile("s_waitcnt lgkmcnt(0)" ::: "memory");
  if (lane == 0) __hip_atomic_fetch_add((LAS unsigned*)(lds + off), 1u, __ATOMIC_RELAXED, __HIP_MEMORY_SCOPE_WORKGROUP);
#pragma unroll 1
  for (unsigned spins = 0; spins < (SPIN_CAP << 10); ++spins) if (*(volatile LAS unsigned*)(lds + off) >= 4 * n) break;
  asm volatile("" ::: "memory");
}
DI void lds_wait_ge(LAS char* lds, unsigned off, unsigned v, int sleep64) {
#pragma unroll 1
  for (unsigned spins = 0; spins < (SPIN_CAP << 6); ++spins) {
    if (*(volatile LAS unsigned*)(lds + off) >= v) break;
    for (int q = 0; q < sleep64; ++q) __builtin_amdgcn_s_sleep(1);
  }
  asm volatile("" ::: "memory");
}
DI void barrier_lds() { asm volatile("s_waitcnt lgkmcnt(0)\n\ts_barrier" ::: "memory"); }
template <int N> DI void wait_vm() { asm volatile("s_waitcnt vmcnt(%0)" :: "i"(N) : "memory"); }
template <int N> DI void wait_lgkm() { asm volatile("s_waitcnt lgkmcnt(%0)" :: "i"(N) : "memory"); }

struct Frag { u32x2 lo, hi; };
DI u32x4 cat(const Frag& f) { return u32x4{f.lo[0], f.lo[1], f.hi[0], f.hi[1]}; }

struct Stream {
  unsigned vo[5];
  unsigned m0s[3];
};
template <int OFF> DI void dma_ld(unsigned vo) {
  asm volatile("buffer_load_dwordx4 %0, s[88:91], s92 offen offset:%1 lds" :: "v"(vo), "i"(OFF) : "memory");
}
DI void set_m0(unsigned v) { asm volatile("s_mov_b32 m0, %0" :: "s"(v) : "memory"); }
DI void set_stream(const void* p, unsigned bytes, unsigned soff0, unsigned step) {
  const u64 a = (u64)p - 4096;
  asm volatile("s_nop 0\n\tv_readfirstlane_b32 s88, %0\n\tv_readfirstlane_b32 s89, %1\n\tv_readfirstlane_b32 s90, %2\n\ts_mov_b32 s91, 0x20000\n\t"
               "v_readfirstlane_b32 s92, %3\n\tv_readfirstlane_b32 s93, %4\n\ts_nop 4"
               :: "v"((unsigned)a), "v"((unsigned)(a >> 32) & 0xffffu), "v"(bytes + 4096), "v"(soff0), "v"(step) : "s88", "s89", "s90", "s91", "s92", "s93", "memory");
}
DI void stream_advance() { asm volatile("s_add_u32 s92, s92, s93" ::: "scc", "memory"); }
DI void seg4(Stream& s, const void* p, unsigned bytes, unsigned soff0, unsigned stride, int lane) {
  set_stream(p, bytes, soff0, 32 * stride);
  const unsigned base = (lane >> 3) * stride + (((lane & 7) ^ (((lane >> 4) & 3) << 1)) << 4) + 4096;
#pragma unroll
  for (int m = 0; m < 4; ++m) s.vo[m] = base + m * (8 * stride - 1024);
  s.vo[4] = OOB;
}
DI void seg5(Stream& s, const void* p, unsigned bytes, unsigned soff0, unsigned stride, int n1, int n2, unsigned alt, int lane) {
  set_stream(p, bytes, soff0, 32 * stride);
#pragma unroll
  for (int m = 0; m < 5; ++m) {
    const int c = 64 * m + lane, row = (c * 6554) >> 16, cc = c - 10 * row;
    const unsigned col = cc < n1 ? cc * 16 : alt + (cc - n1) * 16;
    s.vo[m] = cc < n2 ? row * stride + col + 4096 - (m < 4 ? 1024 * m : 0) : OOB;
  }
}
struct Regs { Frag A[2][5]; u32x4 B[2][4]; };

template <int T, int R, int SET, int RS, int BO> DI void unit_read(Regs& Rg, const unsigned (&ta)[4], unsigned vb) {
  if constexpr (R < 2 * T) {
    constexpr int nt = R >> 1, hi = R & 1;
    if constexpr (T == 4) { if constexpr (hi) tr_rd<RS + 2048>(Rg.A[SET][nt].hi, ta[nt]); else tr_rd<RS>(Rg.A[SET][nt].lo, ta[nt]); }
    else { if constexpr (hi) tr_rd<RS + 2560 + 32 * nt>(Rg.A[SET][nt].hi, ta[0]); else tr_rd<RS + 32 * nt>(Rg.A[SET][nt].lo, ta[0]); }
  } else if constexpr (R < 2 * T + 4) {
    b_rd<BO + (R - 2 * T) * 1024>(Rg.B[SET][R - 2 * T], vb);
  }
}
template <int T, bool SWAP, bool FIRST, int P, int ND, bool RD, int GI>
DI void gap(Acc& acc, Regs& R, const Stream& s, const unsigned (&ta)[4], unsigned vb) {
  constexpr int X = P & 1, Y = X ^ 1, nt = GI >> 2, st = GI & 3;
  if constexpr (GI < 4) wait_lgkm<RD ? 3 : 3 - GI>();
  const u32x4 a = cat(R.A[X][nt]);
  if constexpr (SWAP) mfma_acc<nt, st, FIRST>(acc, R.B[X][st], a); else mfma_acc<nt, st, FIRST>(acc, a, R.B[X][st]);
  if constexpr (ND > 0 && GI == 0) set_m0(s.m0s[P % 3]);
  if constexpr (RD) unit_read<T, GI, Y, ((P + 1) % 3) * SLOT, (P + 1) * 4096>(R, ta, vb);
  if constexpr (ND > 0) {
    if constexpr (GI == 1) dma_ld<0>(s.vo[0]);
    if constexpr (GI == 3) dma_ld<1024>(s.vo[1]);
    if constexpr (GI == 5) dma_ld<2048>(s.vo[2]);
    if constexpr (GI == 7) dma_ld<3072>(s.vo[3]);
    if constexpr (ND == 5 && GI == 8) set_m0(s.m0s[P % 3] + 4096);
    if constexpr (ND == 5 && GI == 9) dma_ld<0>(s.vo[4]);
  }
  if constexpr (st == 3) SB();
}
template <int T, bool SWAP, bool FIRST, int P, int ND, bool RD, int VW, int... GI>
DI void unit_(Acc& acc, Regs& R, const Stream& s, const unsigned (&ta)[4], unsigned vb, std::integer_sequence<int, GI...>) {
  if constexpr (RD) wait_vm<VW>();
  SB();
  (gap<T, SWAP, FIRST, P, ND, RD, GI>(acc, R, s, ta, vb), ...);
  if constexpr (ND > 0) stream_advance();
  SB();
}
template <int T, bool SWAP, bool FIRST, int P, int ND, bool RD = true, int VW = 4>
DI void unit(Acc& acc, Regs& R, const Stream& s, const unsigned (&ta)[4], unsigned vb) { unit_<T, SWAP, FIRST, P, ND, RD, VW>(acc, R, s, ta, vb, std::make_integer_sequence<int, 4 * T>{}); }

template <int T, int... R> DI void first_reads(Regs& Rg, const unsigned (&ta)[4], unsigned vb, std::integer_sequence<int, R...>) { (unit_read<T, R, 0, 0, 0>(Rg, ta, vb), ...); }

template <int T, bool SWAP, int N, int ND, int ND2, int ND3, int NH, class Init, class Next, class Hook, class Tail, class St0, class St1>
DI void pass(Acc& acc, Stream& s, const unsigned (&ta)[4], int lane, Init init, Next next, Hook hook, Tail tail, St0 st0, St1 st1) {
  Regs R;
  unsigned vb = ACT_OFF + lane * 16;
  wait_vm<8>();
  first_reads<T>(R, ta, vb, std::make_integer_sequence<int, 2 * T + 4>{});
  init();
  st0();
#pragma unroll 1
  for (int grp = 0; grp < 3; ++grp) {
    unit<T, SWAP, false, 0, ND>(acc, R, s, ta, vb);
    unit<T, SWAP, false, 1, ND>(acc, R, s, ta, vb);
    if (UNLIKELY(grp == 0)) hook();
    unit<T, SWAP, false, 2, ND>(acc, R, s, ta, vb);
    if (UNLIKELY(grp == 2)) next();
    unit<T, SWAP, false, 3, ND2>(acc, R, s, ta, vb);
    unit<T, SWAP, false, 4, ND2>(acc, R, s, ta, vb);
    unit<T, SWAP, false, 5, ND2>(acc, R, s, ta, vb);
    vb += 6 * 4096;
  }
  if constexpr (N == 21) {
    tail();
    unit<T, SWAP, false, 0, ND3, true, 4 + NH>(acc, R, s, ta, vb);
    unit<T, SWAP, false, 1, ND3, true, ND3 + NH>(acc, R, s, ta, vb);
    unit<T, SWAP, false, 2, ND3, false>(acc, R, s, ta, vb);
  }
  wait_lgkm<0>();
  if constexpr (T == 4) asm volatile("s_nop 7\n\ts_nop 7" : "+v"(acc[0][0]), "+v"(acc[0][1]), "+v"(acc[0][2]), "+v"(acc[0][3]), "+v"(acc[1][0]), "+v"(acc[1][1]), "+v"(acc[1][2]), "+v"(acc[1][3]), "+v"(acc[2][0]), "+v"(acc[2][1]), "+v"(acc[2][2]), "+v"(acc[2][3]), "+v"(acc[3][0]), "+v"(acc[3][1]), "+v"(acc[3][2]), "+v"(acc[3][3]) :: "memory");
  else asm volatile("s_nop 7\n\ts_nop 7" : "+v"(acc[0][0]), "+v"(acc[0][1]), "+v"(acc[0][2]), "+v"(acc[0][3]), "+v"(acc[1][0]), "+v"(acc[1][1]), "+v"(acc[1][2]), "+v"(acc[1][3]), "+v"(acc[2][0]), "+v"(acc[2][1]), "+v"(acc[2][2]), "+v"(acc[2][3]), "+v"(acc[3][0]), "+v"(acc[3][1]), "+v"(acc[3][2]), "+v"(acc[3][3]), "+v"(acc[4][0]), "+v"(acc[4][1]), "+v"(acc[4][2]), "+v"(acc[4][3]) :: "memory");
  SB();
  st1();
}

constexpr unsigned TM_F32 = 15u, TM_BF = 0x00010001u;
struct Tags { unsigned f, b0, b2; };
DI Tags make_tags(unsigned ln) { return Tags{ln & 15u, (ln & 1u) | ((ln & 2u) << 15), ((ln >> 2) & 1u) | ((ln & 8u) << 13)}; }
struct TagCheck {
  unsigned a = ~0u, o = 0u;
  DI void add(unsigned v) { a &= v; o |= v; }
  DI bool mine(unsigned m, unsigned t) const { return (a & m) == t && (o & m) == t; }
};
DI u32x4 ld_sc1(const __amdgpu_buffer_rsrc_t& r, unsigned off) { return __builtin_amdgcn_raw_buffer_load_b128(r, off, 0, 16); }
DI void new_round() { asm volatile("" ::: "memory"); }
DI unsigned pack_tagged(float lo, float hi, unsigned tb) {
  const unsigned a = __float_as_uint(lo) + 0x10000u, b = __float_as_uint(hi) + 0x10000u;
  return (__builtin_amdgcn_perm(b, a, 0x07060302u) & ~TM_BF) | tb;
}
DI u32x4 pack2_tagged(const f32x4& a, const f32x4& b, const Tags& t) { return u32x4{pack_tagged(a[0], a[1], t.b0), cvtpk(a[2], a[3]), pack_tagged(b[0], b[1], t.b2), cvtpk(b[2], b[3])}; }
DI u32x4 hi_halves(const u32x4& a, const u32x4& b) {
  return u32x4{__builtin_amdgcn_perm(a[1], a[0], 0x07060302u), __builtin_amdgcn_perm(a[3], a[2], 0x07060302u), __builtin_amdgcn_perm(b[1], b[0], 0x07060302u), __builtin_amdgcn_perm(b[3], b[2], 0x07060302u)};
}
DI void probe(const __amdgpu_buffer_rsrc_t& r, unsigned off, unsigned m, unsigned t, int sleep64, unsigned& spins) {
#pragma unroll 1
  for (; spins < SPIN_CAP; ++spins) {
    asm volatile("" ::: "memory");
    const unsigned v = __builtin_amdgcn_raw_buffer_load_b32(r, off, 0, 16);
    if (__all((v & m) == t)) return;
    for (int q = 0; q < sleep64; ++q) __builtin_amdgcn_s_sleep(1);
  }
}
DI void st_sc1(const __amdgpu_buffer_rsrc_t& r, unsigned off, const u32x4& v) { __builtin_amdgcn_raw_buffer_store_b128(v, r, off, 0, 16); }

DI void h_epilogue(const Acc& c, u32x4 (&h)[2][4]) {
#pragma unroll
  for (int m = 0; m < 2; ++m)
#pragma unroll
    for (int st = 0; st < 4; ++st) {
      f32x4 p = c[2 * m][st], q = c[2 * m + 1][st];
#pragma unroll
      for (int e = 0; e < 4; ++e) { p[e] = fmaxf(p[e], 0.f); q[e] = fmaxf(q[e], 0.f); }
      p = p * p; q = q * q;
      u32x4 r = {cvtpk(p[0], p[1]), cvtpk(p[2], p[3]), cvtpk(q[0], q[1]), cvtpk(q[2], q[3])};
      pin_v(r);
      h[m][st] = r;
    }
}
DI u32x4 pack2(const f32x4& a, const f32x4& b) { return u32x4{cvtpk(a[0], a[1]), cvtpk(a[2], a[3]), cvtpk(b[0], b[1]), cvtpk(b[2], b[3])}; }

DI void seg_A(Stream& s, int w, int j, const uint16_t* wqkv, const uint16_t* w1, int lane) {
  const void* pa = w < 3 ? (const void*)wqkv : (const void*)w1;
  const unsigned bytes = w < 3 ? DM * 3456 : DM * 9216, stride = w < 3 ? 3456 : 9216;
  const unsigned so = w < 2 ? 1152 + 144 * j + 64 * w : (w == 2 ? 144 * j : (576 * j + 512) * 2);
  const int n1 = w < 2 ? 5 : (w == 2 ? 9 : 8), n2 = w < 2 ? 10 : n1;
  seg5(s, pa, bytes, so, stride, n1, n2, 1152, lane);
#pragma unroll
  for (int q = 0; q < 3; ++q) s.m0s[q] = RING_OFF + w * RING_W + q * SLOT;
}
DI u32x4 mk_srd(const void* p, unsigned bytes) { return u32x4{(unsigned)(u64)p, (unsigned)((u64)p >> 32) & 0xffffu, bytes, 0x00020000u}; }
DI void finish_granule(const u32x4 (&t)[8], int q, int w, int i, int j, int lane, const LAS char* lds, uint16_t* __restrict__ out) {
  const int g = lane >> 4, l15 = lane & 15;
  float v[8] = {0, 0, 0, 0, 0, 0, 0, 0};
#pragma unroll
  for (int a = 0; a < 8; ++a)
#pragma unroll
    for (int e = 0; e < 4; ++e) {
      const unsigned m = (e & 1) ? 0xffff0000u : 0xfffe0000u;
      v[2 * e] += __uint_as_float((t[a][e] << 16) & m); v[2 * e + 1] += __uint_as_float(t[a][e] & m);
    }
#pragma unroll
  for (int h = 0; h < 2; ++h) {
    const int dt = q == 1 ? 32 + w : (q == 0 ? 0 : 16) + 4 * w + 2 * (j >> 2) + h, st = q == 1 ? 2 * (j & 1) + h : (j & 3);
    const unsigned xo = q == 1 ? XS_OFF + 8192 + h * 512 + l15 * 32 + g * 8 : XS_OFF + (q == 0 ? w : 4 + w) * 1024 + l15 * 64 + h * 32 + g * 8;
    const u32x2 xr2 = *(const LAS u32x2*)(lds + xo), bb = *(const LAS u32x2*)(lds + B2_OFF + (16 * dt + 4 * g) * 2);
    const u32x2 r = {cvtpk(v[4 * h] + bflo(xr2[0]) + bflo(bb[0]), v[4 * h + 1] + bfhi(xr2[0]) + bfhi(bb[0])),
                     cvtpk(v[4 * h + 2] + bflo(xr2[1]) + bflo(bb[1]), v[4 * h + 3] + bfhi(xr2[1]) + bfhi(bb[1]))};
    *(GAS u32x2*)(out + (size_t)(64 * i + 16 * st + l15) * DM + 16 * dt + 4 * g) = r;
  }
}

__global__ __launch_bounds__(320, 2) __attribute__((amdgpu_num_sgpr(88))) void fused_block_kernel(
    const uint16_t* __restrict__ x, const uint16_t* __restrict__ wqkv, const uint16_t* __restrict__ wo, const uint16_t* __restrict__ w1, const uint16_t* __restrict__ b1,
    const uint16_t* __restrict__ w2, const uint16_t* __restrict__ b2, uint16_t* __restrict__ out, char* __restrict__ ws) {
  extern __shared__ __attribute__((aligned(16))) char smem[];
  LAS char* lds = (LAS char*)smem;
  const int tid = threadIdx.x, lane = tid & 63;
  const int w = rfl(tid >> 6);
  const int j = blockIdx.x & 7, i = blockIdx.x >> 3;
  const int g = lane >> 4, l15 = lane & 15;
  GAS char* wsg = (GAS char*)ws;
  const __amdgpu_buffer_rsrc_t wsr = __builtin_amdgcn_make_buffer_rsrc((void*)ws, 0, WS_END, 0x00020000);
  u64 pc0;
  asm volatile("s_getpc_b64 %0" : "=s"(pc0));
  const unsigned ep = __hip_atomic_load((const GAS unsigned*)(wsg + WS_EPOCH), RLX_AGENT);

  if (w == 4) {
    auto touch = [&](const u32x4& d, unsigned off) { asm volatile("buffer_load_dword %0, %1, 0 offen lds" :: "v"(off), "s"(d) : "memory"); };
    set_m0(SCR_OFF); asm volatile("s_nop 0");
    {
      const unsigned ln0 = 12 * i + (lane < 12 ? lane : 0);
      const u64 a = (pc0 & ~127ull) + 128ull * (ln0 < CODE_LINES ? ln0 : 0);
      asm volatile("global_load_lds_dword %0, off" :: "v"(a) : "memory");
    }
    {
      const u32x4 d = mk_srd(wqkv, DM * 3456);
#pragma unroll
      for (int q = 0; q < 2; ++q) { const int t = lane + 64 * q; touch(d, t < 108 ? (18 * i + t / 6) * 3456 + ((t % 6) >> 1) * 1152 + ((144 * j) & ~127) + ((t % 6) & 1) * 128 : OOB); }
      touch(mk_srd(w1, DM * 9216), lane < 18 ? (18 * i + lane) * 9216 + 1152 * j + 1024 : OOB);
    }
    {
      const u32x4 d1 = mk_srd(b1, FF * 2), d2 = mk_srd(b2, DM * 2);
#pragma unroll
      for (int q = 0; q < 2; ++q) {
        const int sg = (lane >> 3) + 8 * q, wv = sg / 3, ln = sg % 3;
        const unsigned o = sg < 12 ? (576 * j + (ln == 2 ? 512 : 128 * wv + 64 * ln)) * 2 + (lane & 7) * 16 : OOB;
        set_m0(B1_OFF + q * 1024);
        asm volatile("s_nop 0\n\tbuffer_load_dwordx4 %0, %1, 0 offen lds" :: "v"(o), "s"(d1) : "memory");
      }
      set_m0(B2_OFF);
      asm volatile("s_nop 0\n\tbuffer_load_dwordx4 %0, %1, 0 offen lds" :: "v"((unsigned)lane * 16), "s"(d2) : "memory");
      set_m0(B2_OFF + 1024);
      asm volatile("s_nop 0\n\tbuffer_load_dwordx4 %0, %1, 0 offen lds" :: "v"(lane < 8 ? 1024u + lane * 16 : OOB), "s"(d2) : "memory");
    }
    {
      unsigned vo4[4][5];
#pragma unroll
      for (int wv = 0; wv < 4; ++wv) {
        Stream s4;
        seg_A(s4, wv, j, wqkv, w1, lane);
#pragma unroll
        for (int m = 0; m < 5; ++m) vo4[wv][m] = s4.vo[m];
      }
#pragma unroll
      for (int u = 0; u < 3; ++u) {
        if (u == 2) wait_vm<40>();
#pragma unroll
        for (int wv = 0; wv < 4; ++wv) {
          const unsigned stride = wv < 3 ? 3456 : 9216;
          set_stream(wv < 3 ? (const void*)wqkv : (const void*)w1, wv < 3 ? DM * 3456 : DM * 9216,
                     (wv < 2 ? 1152 + 144 * j + 64 * wv : (wv == 2 ? 144 * j : (576 * j + 512) * 2)) + u * 32 * stride, 32 * stride);
          const unsigned lb = RING_OFF + wv * RING_W + u * SLOT;
          set_m0(lb); asm volatile("s_nop 0");
          dma_ld<0>(vo4[wv][0]); dma_ld<1024>(vo4[wv][1]); dma_ld<2048>(vo4[wv][2]); dma_ld<3072>(vo4[wv][3]);
          set_m0(lb + 4096); asm volatile("s_nop 0");
          dma_ld<0>(vo4[wv][4]);
        }
      }
    }
    if (lane < 6) *(LAS unsigned*)(lds + PQF_OFF + lane * 4) = 0;
    wait_vm<40>();
    barrier_lds();
    wait_vm<0>();
    const unsigned ln = rfl(ep) + 1;
    const Tags tg = make_tags(ln);
    if (lane == 0) *(volatile LAS unsigned*)(lds + PQF_OFF + 20) = 1;
    constexpr int PE[9] = {0, 1, 1, 2, 2, 3, 3, 4, 4}, PK_[9] = {0, 0, 1, 0, 1, 1, 2, 1, 2};
    unsigned oz[3][2], ok_[9][2];
    u32x4 msk[9], mz[3];
#pragma unroll
    for (int ks = 0; ks < 3; ++ks)
#pragma unroll
      for (int half = 0; half < 2; ++half) {
        const int db = 32 * ks + 16 * half + 4 * g, hp = db >= 36 ? 1 : 0, zc = hp ? 40 : 76;
        int dp = db - 32 * hp; dp = dp > 36 ? 36 : dp;
        oz[ks][half] = WS_KVF + (j * 18 + hp * 9 + (dp >> 4) * 3 + (zc >> 4) - 2) * 1024 + ((dp & 15) >> 2) * 256 + (zc & 15) * 16;
#pragma unroll
        for (int q = 0; q < 2; ++q) {
          const bool lo = (unsigned)(db + 2 * q - 18 * (l15 & 3)) < 18u, hi = (unsigned)(db + 2 * q + 1 - 18 * (l15 & 3)) < 18u;
          mz[ks][2 * half + q] = (lo ? 0xffffu : 0u) | (hi ? 0xffff0000u : 0u);
        }
      }
    pin_v(mz[0]); pin_v(mz[1]); pin_v(mz[2]);
#pragma unroll
    for (int p = 0; p < 9; ++p) {
      const int e = 16 * PE[p] + l15, He = (e * 3641) >> 16, hp = (He >> 1) & 1;
      int c = 40 + e - 32 * hp; c = c > 79 ? 79 : c;
#pragma unroll
      for (int half = 0; half < 2; ++half) {
        const int db = 32 * PK_[p] + 16 * half + 4 * g;
        int dp = db - 32 * hp; dp = dp < 0 ? 0 : (dp > 36 ? 36 : dp);
        ok_[p][half] = WS_KVF + (j * 18 + hp * 9 + (dp >> 4) * 3 + (c >> 4) - 2) * 1024 + ((dp & 15) >> 2) * 256 + (c & 15) * 16;
#pragma unroll
        for (int q = 0; q < 2; ++q) {
          const bool lo = ((unsigned)(db + 2 * q - 18 * He) < 18u) & (e < 72), hi = ((unsigned)(db + 2 * q + 1 - 18 * He) < 18u) & (e < 72);
          msk[p][2 * half + q] = (lo ? 0xffffu : 0u) | (hi ? 0xffff0000u : 0u);
        }
      }
      pin_v(msk[p]); pin_v(ok_[p][0]); pin_v(ok_[p][1]);
    }
    lds_wait_ge(lds, PQF_OFF + 4, 1, 8);
    for (int q = 0; q < KV1_DELAY; ++q) __builtin_amdgcn_s_sleep(8);
    if (i < 18) {
      __builtin_amdgcn_s_setprio(1);
      u32x4 tv[32];
#pragma unroll 1
      for (unsigned spins = 0; spins < SPIN_CAP; ++spins) {
        new_round();
        TagCheck tc;
        probe(wsr, WS_KVP + ((j * 32 + (lane & 31)) * 18 + i) * 1024, TM_F32, tg.f, 6, spins);
#pragma unroll
        for (int q = 0; q < 32; ++q) tv[q] = ld_sc1(wsr, WS_KVP + ((j * 32 + q) * 18 + i) * 1024 + lane * 16);
#pragma unroll
        for (int q = 0; q < 32; ++q) tc.add(tv[q][0]);
        if (__all(tc.mine(TM_F32, tg.f))) break;
        __builtin_amdgcn_s_sleep(8);
      }
      f32x4 sum = {0, 0, 0, 0};
#pragma unroll
      for (int q = 0; q < 32; ++q) { u32x4 v = tv[q]; v[0] &= ~TM_F32; sum += __builtin_bit_cast(f32x4, v); }
      const unsigned s01 = cvtpk(sum[0], sum[1]), s23 = cvtpk(sum[2], sum[3]);
      st_sc1(wsr, WS_KVF + (j * 18 + i) * 1024 + lane * 16, u32x4{(s01 << 16) | tg.f, s01 & 0xffff0000u, s23 << 16, s23 & 0xffff0000u});
      __builtin_amdgcn_s_setprio(0);
    }
    lds_wait_ge(lds, PQF_OFF, 1, 8);
#pragma unroll
    for (int st = 0; st < 4; ++st) {
      LAS u32x2* pp = (LAS u32x2*)(lds + ACT_OFF + ((PQ_KS + 2) * 4 + st) * 1024 + lane * 16);
      const u32x2 v = *pp;
      const f32x4 y = phi4(f32x4{bflo(v[0]), bfhi(v[0]), bflo(v[1]), bfhi(v[1])});
      *pp = u32x2{cvtpk(y[0], y[1]), cvtpk(y[2], y[3])};
    }
    for (int q = 0; q < (i < 18 ? 0 : KV_EXTRA); ++q) __builtin_amdgcn_s_sleep(8);
    {
      u32x4 gk[9][2], gz[3][2];
#pragma unroll 1
      for (unsigned spins = 0; spins < SPIN_CAP; ++spins) {
        new_round();
        TagCheck tc;
        probe(wsr, WS_KVF + (j * 18 + (lane < 18 ? lane : 0)) * 1024, TM_F32, tg.f, 6, spins);
#pragma unroll
        for (int ks = 0; ks < 3; ++ks)
#pragma unroll
          for (int half = 0; half < 2; ++half) gz[ks][half] = ld_sc1(wsr, oz[ks][half]);
#pragma unroll
        for (int p = 0; p < 9; ++p)
#pragma unroll
          for (int half = 0; half < 2; ++half) gk[p][half] = ld_sc1(wsr, ok_[p][half]);
#pragma unroll
        for (int ks = 0; ks < 3; ++ks) { tc.add(gz[ks][0][0]); tc.add(gz[ks][1][0]); }
#pragma unroll
        for (int p = 0; p < 9; ++p) { tc.add(gk[p][0][0]); tc.add(gk[p][1][0]); }
        if (__all(tc.mine(TM_F32, tg.f))) break;
        __builtin_amdgcn_s_sleep(8);
      }
      u32x4 pq[3][4];
#pragma unroll
      for (int ks = 0; ks < 3; ++ks)
#pragma unroll
        for (int st = 0; st < 4; ++st) pq[ks][st] = *(const LAS u32x4*)(lds + ACT_OFF + ((PQ_KS + ks) * 4 + st) * 1024 + lane * 16);
      f32x4 rd[4];
#pragma unroll
      for (int st = 0; st < 4; ++st) {
        f32x4 d = {0, 0, 0, 0};
#pragma unroll
        for (int ks = 0; ks < 3; ++ks) d = mfma_b(hi_halves(gz[ks][0], gz[ks][1]) & mz[ks], pq[ks][st], d);
#pragma unroll
        for (int r = 0; r < 4; ++r) rd[st][r] = __builtin_amdgcn_rcpf(d[r] + 1e-6f);
      }
      f32x4 num[4];
#pragma unroll
      for (int p = 0; p < 9; ++p) {
        const int et = PE[p], ks = PK_[p];
        const bool first = p == 0 || PE[p - 1] != et, last = p == 8 || PE[p + 1] != et;
        const u32x4 fk = hi_halves(gk[p][0], gk[p][1]) & msk[p];
#pragma unroll
        for (int st = 0; st < 4; ++st) {
          num[st] = mfma_b(fk, pq[ks][st], first ? f32x4{0, 0, 0, 0} : num[st]);
          if (last) {
            const f32x4 q = rd[st];
            float a0, a1, a2, a3;
            if (et == 0) { a0 = a1 = a2 = a3 = q[0]; }
            else if (et == 1) { a0 = a1 = g == 0 ? q[0] : q[1]; a2 = a3 = q[1]; }
            else if (et == 2) { a0 = a1 = a2 = a3 = g == 0 ? q[1] : q[2]; }
            else if (et == 3) { a0 = a1 = g <= 1 ? q[2] : q[3]; a2 = a3 = g == 0 ? q[2] : q[3]; }
            else { a0 = a1 = a2 = a3 = q[3]; }
            *(LAS u32x2*)(lds + ACT_OFF + ((PQ_KS + (et >> 1)) * 4 + st) * 1024 + lane * 16 + (et & 1) * 8) =
                u32x2{cvtpk(num[st][0] * a0, num[st][1] * a1), cvtpk(num[st][2] * a2, num[st][3] * a3)};
          }
        }
      }
    }
    asm volatile("s_waitcnt lgkmcnt(0)" ::: "memory");
    if (lane == 0) *(volatile LAS unsigned*)(lds + PQF_OFF + 12) = 1;
    return;
  }

  const int xr = lane >> 3, xc = lane & 7;
  u32x4 xv[18];
  {
    const GAS char* xp = (const GAS char*)(x + (size_t)(64 * i + 16 * w + xr) * DM + 8 * xc);
    const GAS char* xq = xp + 8 * DM * 2;
#define XLD(k, p) asm volatile("global_load_dwordx4 %0, %1, off offset:%2" : "=v"(xv[k]) : "v"(p), "i"(((k) % 9) * 128) : "memory")
    XLD(0, xp); XLD(1, xp); XLD(2, xp); XLD(3, xp); XLD(4, xp); XLD(5, xp); XLD(6, xp); XLD(7, xp); XLD(8, xp);
    XLD(9, xq); XLD(10, xq); XLD(11, xq); XLD(12, xq); XLD(13, xq); XLD(14, xq); XLD(15, xq); XLD(16, xq); XLD(17, xq);
  }
  Stream s;
  seg_A(s, w, j, wqkv, w1, lane);
  stream_advance(); stream_advance(); stream_advance();
  auto layernorm8 = [&](const u32x4* xg, int r8) {
    const bf16x2 ones = __builtin_bit_cast(bf16x2, 0x3f803f80u);
    float sm[2] = {0.f, 0.f}, sq[2] = {0.f, 0.f};
#pragma unroll
    for (int k = 0; k < 9; ++k)
#pragma unroll
      for (int e = 0; e < 4; ++e) {
        const unsigned u = xg[k][e];
        const bf16x2 b = __builtin_bit_cast(bf16x2, u);
        sm[e & 1] = __builtin_amdgcn_fdot2_f32_bf16(b, ones, sm[e & 1], false);
        sq[e & 1] = __builtin_amdgcn_fdot2_f32_bf16(b, b, sq[e & 1], false);
      }
    const float mean = sum8(sm[0] + sm[1]) * (1.f / DM), ex2 = sum8(sq[0] + sq[1]) * (1.f / DM);
    const float rs = __builtin_amdgcn_rsqf(fmaxf(ex2 - mean * mean, 0.f) + 1e-5f), nb = -mean * rs;
    const f32x2 r2 = {rs, rs}, n2 = {nb, nb};
    const unsigned wa = ACT_OFF + w * 1024 + (xc >> 2) * 4096 + (xr + 8 * r8 + 16 * ((2 * xc) & 3)) * 16 + ((xc >> 1) & 1) * 8;
#pragma unroll
    for (int k = 0; k < 9; ++k) {
      const f32x2 a = f32x2{bflo(xg[k][0]), bfhi(xg[k][0])} * r2 + n2, b = f32x2{bflo(xg[k][1]), bfhi(xg[k][1])} * r2 + n2;
      const f32x2 c = f32x2{bflo(xg[k][2]), bfhi(xg[k][2])} * r2 + n2, d = f32x2{bflo(xg[k][3]), bfhi(xg[k][3])} * r2 + n2;
      *(LAS u32x2*)(lds + wa + k * 8192) = u32x2{cvtpk(a[0], a[1]), cvtpk(b[0], b[1])};
      *(LAS u32x2*)(lds + wa + k * 8192 + 256) = u32x2{cvtpk(c[0], c[1]), cvtpk(d[0], d[1])};
    }
    if (w == (j & 3) && (xc >> 2) == (j >> 2)) {
#pragma unroll
      for (int k = 0; k < 8; ++k) *(LAS u32x4*)(lds + XS_OFF + k * 1024 + (xr + 8 * r8) * 64 + (xc & 3) * 16) = xg[k];
    }
    if ((w >> 1) == (j & 1) && (xc >> 1) == (j >> 1)) *(LAS u32x4*)(lds + XS_OFF + 8192 + (w & 1) * 512 + (xr + 8 * r8) * 32 + (xc & 1) * 16) = xg[8];
  };
  asm volatile("s_waitcnt vmcnt(9)" : "+v"(xv[0]), "+v"(xv[1]), "+v"(xv[2]), "+v"(xv[3]), "+v"(xv[4]), "+v"(xv[5]), "+v"(xv[6]), "+v"(xv[7]), "+v"(xv[8]) :: "memory");
  layernorm8(xv, 0);
  asm volatile("s_waitcnt vmcnt(0)" : "+v"(xv[9]), "+v"(xv[10]), "+v"(xv[11]), "+v"(xv[12]), "+v"(xv[13]), "+v"(xv[14]), "+v"(xv[15]), "+v"(xv[16]), "+v"(xv[17]) :: "memory");
  layernorm8(xv + 9, 1);
  const unsigned ln = rfl(ep) + 1;
  const Tags tg = make_tags(ln);
  unsigned ta4[4], ta5[4];
  {
    const int q = l15 >> 2, p = lane & 3, row = 4 * g + q;
    const unsigned rb = RING_OFF + w * RING_W;
#pragma unroll
    for (int nt = 0; nt < 4; ++nt) ta4[nt] = rb + row * 128 + ((nt ^ ((row >> 1) & 3)) * 32) + 8 * p;
    ta5[0] = rb + row * 160 + 8 * p; ta5[1] = ta5[2] = ta5[3] = 0;
  }
  barrier_lds();

  Acc acc;
  u32x4 hA[2][4], hB1[2][4], hB2[2][4];
  auto none = []() {};
  auto to_B1 = [&]() { seg4(s, w1, DM * 9216, (576 * j + 128 * w) * 2, 9216, lane); };
  auto touch = [&](const u32x4& d, unsigned off) {
    set_m0(SCR_OFF);
    asm volatile("s_nop 0\n\tbuffer_load_dword %0, %1, 0 offen lds" :: "v"(off), "s"(d) : "memory");
  };
  auto touch_w1 = [&]() { touch(mk_srd(w1, DM * 9216), tid < 144 ? (18 * i + (tid >> 3)) * 9216 + 1152 * j + (tid & 7) * 128 : OOB); };
  auto touch_w2 = [&]() {
    const u64 p = w < 3 ? (u64)w2 : (u64)wo;
    const u32x4 d = {rfl_s((unsigned)p), rfl_s((unsigned)(p >> 32) & 0xffffu), rfl_s(w < 3 ? FF * 1152 : DM * 1152), 0x00020000u};
    touch(d, w < 3 ? (tid < 162 ? (576 * j + 18 * i) * 1152 + tid * 128 : OOB) : (lane < 21 && i * 21 + lane < 648 ? 72 * j * 1152 + (i * 21 + lane) * 128 : OOB));
  };
  if (w < 2) {
    pass<5, true, 18, 5, 5, 0, 0>(acc, s, ta5, lane, [&]() { acc_zero<5>(acc); lds_wait_ge(lds, PQF_OFF + 20, 1, 0); }, to_B1, none, none, none, touch_w1);
    Acc& c = acc;
    u32x4 PK[3][2], V[3][2];
#pragma unroll
    for (int p = 0; p < 2; ++p) {
#pragma unroll
      for (int nt = 0; nt < 3; ++nt) {
        PK[nt][p] = pack2(phi4(c[nt][2 * p]), phi4(c[nt][2 * p + 1]));
        u32x4 v = pack2(c[nt + 2][2 * p], c[nt + 2][2 * p + 1]);
        if ((nt == 2 && w == 0 && l15 == 12) || (nt == 0 && w == 1 && l15 == 8)) v = u32x4{0x3f803f80u, 0x3f803f80u, 0x3f803f80u, 0x3f803f80u};
        V[nt][p] = v;
      }
    }
    const unsigned pbase = WS_KVP + ((j * 32 + i) * 18 + w * 9) * 1024 + lane * 16;
    int t = 0;
#pragma unroll
    for (int dn = 0; dn < 3; ++dn)
#pragma unroll
      for (int en = 0; en < 3; ++en) {
        f32x4 d = mfma_b(PK[dn][0], V[en][0], f32x4{0, 0, 0, 0});
        d = mfma_b(PK[dn][1], V[en][1], d);
        { u32x4 dv = __builtin_bit_cast(u32x4, d); dv[0] = (dv[0] & ~TM_F32) | tg.f; st_sc1(wsr, pbase + t * 1024, dv); }
        ++t;
      }
    if (lane == 0) *(volatile LAS unsigned*)(lds + PQF_OFF + 4) = 1;
  } else {
    pass<5, false, 18, 5, 5, 0, 0>(acc, s, ta5, lane, [&]() { if (w == 3) acc_bias(acc, lds + B1_OFF + w * 384 + 256 + g * 8); else acc_zero<4>(acc); acc[4][0] = acc[4][1] = acc[4][2] = acc[4][3] = f32x4{0.f, 0.f, 0.f, 0.f}; lds_wait_ge(lds, PQF_OFF + 20, 1, 0); },
                                   to_B1, none, none, none, touch_w1);
    if (w == 2) {
      Acc& c = acc;
#pragma unroll
      for (int st = 0; st < 4; ++st) {
#pragma unroll
        for (int nt = 0; nt < 4; ++nt) c[nt][st] = phi4(c[nt][st]);
        *(LAS u32x4*)(lds + ACT_OFF + ((PQ_KS + 0) * 4 + st) * 1024 + lane * 16) = pack2(c[0][st], c[1][st]);
        *(LAS u32x4*)(lds + ACT_OFF + ((PQ_KS + 1) * 4 + st) * 1024 + lane * 16) = pack2(c[2][st], c[3][st]);
        *(LAS u32x4*)(lds + ACT_OFF + ((PQ_KS + 2) * 4 + st) * 1024 + lane * 16) = u32x4{cvtpk(c[4][st][0], c[4][st][1]), cvtpk(c[4][st][2], c[4][st][3]), 0u, 0u};
      }
      asm volatile("s_waitcnt lgkmcnt(0)" ::: "memory");
      if (lane == 0) *(volatile LAS unsigned*)(lds + PQF_OFF) = 1;
    } else {
      h_epilogue(acc, hA);
    }
  }
  {
    auto nxt = [&]() { seg4(s, w1, DM * 9216, (576 * j + 128 * w + 64) * 2, 9216, lane); };
    pass<4, false, 18, 4, 4, 0, 0>(acc, s, ta4, lane, [&]() { acc_bias(acc, lds + B1_OFF + w * 384 + g * 8); }, nxt, none, none, none, none);
    h_epilogue(acc, hB1);
  }
  {
    auto nxt = [&]() { seg5(s, w2, FF * 1152, 576 * j * 1152 + w * 128, 1152, 8, 10, (8 - w) * 128 + 32 * w, lane); };
    pass<4, false, 18, 4, 5, 0, 0>(acc, s, ta4, lane, [&]() { acc_bias(acc, lds + B1_OFF + w * 384 + 128 + g * 8); }, nxt, none, none, none, touch_w2);
    h_epilogue(acc, hB2);
  }
  sync4(lds, PQF_OFF + 16, 1, lane);
  {
    const unsigned a0 = ACT_OFF + 4 * w * 4096 + lane * 16, a3 = ACT_OFF + 16 * 4096 + lane * 16;
#define WRH(m, st) lds_wr128<(m) * 4096 + (st) * 1024>(a0, hB1[m][st]); lds_wr128<(2 + (m)) * 4096 + (st) * 1024>(a0, hB2[m][st])
    WRH(0, 0); WRH(0, 1); WRH(0, 2); WRH(0, 3); WRH(1, 0); WRH(1, 1); WRH(1, 2); WRH(1, 3);
#define WRA(m, st) lds_wr128<(m) * 4096 + (st) * 1024>(a3, hA[m][st])
    if (w == 3) { WRA(0, 0); WRA(0, 1); WRA(0, 2); WRA(0, 3); WRA(1, 0); WRA(1, 1); WRA(1, 2); WRA(1, 3); }
  }
  sync4(lds, PQF_OFF + 16, 2, lane);
  {
    auto nxt = [&]() {
      seg5(s, wo, (72 * j + 72) * 1152, 72 * j * 1152 + w * 128, 1152, 8, 10, (8 - w) * 128 + 32 * w, lane);
      lds_wait_ge(lds, PQF_OFF + 12, 1, 1);
    };
    auto tail = [&]() { seg4(s, w2, FF * 1152, 576 * j * 1152 + (4 + w) * 128, 1152, lane); };
    pass<5, false, 21, 5, 5, 4, 0>(acc, s, ta5, lane, [&]() { acc_zero<5>(acc); }, nxt, none, tail, none, none);
    Acc& c = acc;
    const unsigned sb = WS_OS + (((i * 8 + j) * 2 + 0) * 4 + w) * 8 * 1024 + lane * 16;
#pragma unroll
    for (int d = 0; d < 8; ++d) st_sc1(wsr, sb + d * 1024, pack2_tagged(c[2 * (d >> 2)][d & 3], c[2 * (d >> 2) + 1][d & 3], tg));
#pragma unroll
    for (int p = 0; p < 2; ++p) st_sc1(wsr, WS_OX + (((i * 8 + j) * 4 + w) * 2 + p) * 1024 + lane * 16, pack2_tagged(c[4][2 * p], c[4][2 * p + 1], tg));
  }
  u32x4 t1[8], tx[8];
  const bool ninth = w == (j >> 1);
  {
    const u32x4 d_ws = mk_srd(ws, WS_END);
    auto nxt = [&]() { seg4(s, wo, (72 * j + 72) * 1152, 72 * j * 1152 + (4 + w) * 128, 1152, lane); };
    auto tail = [&]() {
#pragma unroll
      for (int q = 0; q < 8; ++q) {
        const unsigned o1 = WS_OS + ((((i * 8 + q) * 2 + 0) * 4 + w) * 8 + j) * 1024 + lane * 16;
        asm volatile("buffer_load_dwordx4 %0, %1, %2, 0 offen sc1" : "=v"(t1[q]) : "v"(o1), "s"(d_ws) : "memory");
        const unsigned ox = ninth ? WS_OX + (((i * 8 + q) * 4 + w) * 2 + (j & 1)) * 1024 + lane * 16 : OOB;
        asm volatile("buffer_load_dwordx4 %0, %1, %2, 0 offen sc1" : "=v"(tx[q]) : "v"(ox), "s"(d_ws) : "memory");
      }
    };
    pass<4, false, 21, 4, 4, 0, 16>(acc, s, ta4, lane, [&]() { acc_zero<4>(acc); }, nxt, none, tail, none, none);
    Acc& c = acc;
    const unsigned sb = WS_OS + (((i * 8 + j) * 2 + 1) * 4 + w) * 8 * 1024 + lane * 16;
#pragma unroll
    for (int d = 0; d < 8; ++d) st_sc1(wsr, sb + d * 1024, pack2_tagged(c[2 * (d >> 2)][d & 3], c[2 * (d >> 2) + 1][d & 3], tg));
  }
  {
    asm volatile("s_waitcnt vmcnt(8)" : "+v"(t1[0]), "+v"(t1[1]), "+v"(t1[2]), "+v"(t1[3]), "+v"(t1[4]), "+v"(t1[5]), "+v"(t1[6]), "+v"(t1[7]),
                 "+v"(tx[0]), "+v"(tx[1]), "+v"(tx[2]), "+v"(tx[3]), "+v"(tx[4]), "+v"(tx[5]), "+v"(tx[6]), "+v"(tx[7]) :: "memory");
#pragma unroll 1
    for (int round = 0; round < 2; ++round) {
#pragma unroll 1
      for (unsigned spins = 0; spins < SPIN_CAP; ++spins) {
        new_round();
        if (round == 1 || spins > 0) {
          if (spins > 0) probe(wsr, WS_OS + ((((i * 8 + (lane & 7)) * 2 + round) * 4 + w) * 8 + j) * 1024, TM_BF, tg.b0, 1, spins);
#pragma unroll
          for (int q = 0; q < 8; ++q) {
            t1[q] = ld_sc1(wsr, WS_OS + ((((i * 8 + q) * 2 + round) * 4 + w) * 8 + j) * 1024 + lane * 16);
            if (round == 0) tx[q] = ld_sc1(wsr, ninth ? WS_OX + (((i * 8 + q) * 4 + w) * 2 + (j & 1)) * 1024 + lane * 16 : OOB);
          }
        }
        TagCheck tc, t2;
#pragma unroll
        for (int q = 0; q < 8; ++q) { tc.add(t1[q][0]); t2.add(t1[q][2]); if (round == 0 && ninth) { tc.add(tx[q][0]); t2.add(tx[q][2]); } }
        if (__all(tc.mine(TM_BF, tg.b0) && t2.mine(TM_BF, tg.b2))) break;
      }
      finish_granule(t1, round == 0 ? 0 : 2, w, i, j, lane, lds, out);
      if (round == 0 && ninth) finish_granule(tx, 1, w, i, j, lane, lds, out);
    }
  }
  if (blockIdx.x == 0 && tid == 0) __hip_atomic_store((GAS unsigned*)(wsg + WS_EPOCH), ln, RLX_AGENT);
}

extern "C" void kernel_launch(void* const* d_in, const int* in_sizes, int n_in, void* d_out, int out_size, void* d_ws, size_t ws_size, hipStream_t stream) {
  (void)in_sizes; (void)n_in; (void)out_size; (void)ws_size;
  (void)hipFuncSetAttribute((const void*)fused_block_kernel, hipFuncAttributeMaxDynamicSharedMemorySize, LDS_BYTES);
  fused_block_kernel<<<256, 320, LDS_BYTES, stream>>>((const uint16_t*)d_in[0], (const uint16_t*)d_in[1], (const uint16_t*)d_in[2], (const uint16_t*)d_in[4],
                                                      (const uint16_t*)d_in[5], (const uint16_t*)d_in[6], (const uint16_t*)d_in[7], (uint16_t*)d_out, (char*)d_ws);
}
```
